```python
import torch

def _softmax(x, axis=-1):
    return torch.softmax(x, dim=axis)
def _gelu(x):
    return torch.nn.functional.gelu(x, approximate="tanh")
def _silu(x):
    return torch.nn.functional.silu(x)
def _sigmoid(x):
    return torch.sigmoid(x)
def _layernorm(x, axis=-1, eps=1e-5):
    m = x.mean(dim=axis, keepdim=True)
    v = x.var(dim=axis, keepdim=True, unbiased=False)
    return (x - m) / torch.sqrt(v + eps)
def _scatter_add(tgt, idx, src, axis=0):
    return tgt.clone().index_add_(axis, idx, src)
def _segment_sum(src, seg, n):
    out = torch.zeros((n,) + src.shape[1:], dtype=src.dtype)
    return out.index_add_(0, seg, src)
def _pad(x, pw):
    flat = tuple(v for pair in reversed(pw) for v in pair)
    return torch.nn.functional.pad(x, flat)

kernel_name = 'standalone__rmsnorm__sampling'

def setup_inputs():

    _w = (lambda a: (lambda t: t.to(torch.bfloat16) if t.is_floating_point() else t)(torch.as_tensor(a)))
    B, S, D = 512, 32, 640
    return {
        "x": _w((lambda *s: torch.randn(*s, dtype=torch.bfloat16) * 0.05)(B, S, D)),
        "g": _w((lambda *s: torch.randn(*s, dtype=torch.bfloat16) * 0.05)(D) + 1.0),
    }

def reference(x, g):
    rms = torch.sqrt((x * x).mean(axis=-1, keepdims=True) + 1e-6)
    return x / rms * g
```

```cpp
#include <hip/hip_runtime.h>
#include <stdint.h>

typedef unsigned int u32x4 __attribute__((ext_vector_type(4)));
typedef float f32x2 __attribute__((ext_vector_type(2)));
typedef __bf16 bf16x2 __attribute__((ext_vector_type(2)));

static constexpr int D = 640;
static constexpr int ROWB = D * 2;
static constexpr int WPB = 16;
static constexpr int EARLY = 3;
static constexpr int NT = 2;

template <int CTRL>
__device__ __forceinline__ float dpp_ror_add(float v) {
    int t = __builtin_amdgcn_update_dpp(0, __builtin_bit_cast(int, v), CTRL, 0xf, 0xf, false);
    return v + __builtin_bit_cast(float, t);
}
__device__ __forceinline__ float row16_allsum(float v) {
    v = dpp_ror_add<0x128>(v);
    v = dpp_ror_add<0x124>(v);
    v = dpp_ror_add<0x122>(v);
    v = dpp_ror_add<0x121>(v);
    return v;
}
__device__ __forceinline__ float lo_f(unsigned u) { return __builtin_bit_cast(float, u << 16); }
__device__ __forceinline__ float hi_f(unsigned u) { return __builtin_bit_cast(float, u & 0xffff0000u); }
__device__ __forceinline__ unsigned pack_bf16(f32x2 v) {
    bf16x2 r;
    r[0] = (__bf16)v[0];
    r[1] = (__bf16)v[1];
    return __builtin_bit_cast(unsigned, r);
}

__global__ __launch_bounds__(WPB * 64) void rmsnorm640(const void* __restrict__ x, const void* __restrict__ g,
                                                       void* __restrict__ out, unsigned nbytes, unsigned wstride) {
    const int tid = threadIdx.x;
    const unsigned c16 = (tid & 15) * 16;
    const unsigned lane_off = ((tid >> 4) & 3) * ROWB + c16 + blockIdx.x * (4 * ROWB);
    asm volatile("" ::"v"(lane_off), "s"(x), "s"(g), "s"(out), "s"(nbytes), "s"(wstride));
    auto rx = __builtin_amdgcn_make_buffer_rsrc((void*)x, 0, nbytes, 0x00020000);
    auto ro = __builtin_amdgcn_make_buffer_rsrc(out, 0, nbytes, 0x00020000);
    auto rg = __builtin_amdgcn_make_buffer_rsrc((void*)g, 0, ROWB, 0x00020000);
    const int wave = __builtin_amdgcn_readfirstlane(tid >> 6);
    const unsigned voff = lane_off + wave * wstride;

    u32x4 xv[5];
#pragma unroll
    for (int j = 0; j < 5; ++j) xv[j] = __builtin_amdgcn_raw_buffer_load_b128(rx, voff + j * 256, 0, NT);
    asm volatile("" ::: "memory");
    __builtin_amdgcn_sched_barrier(0);

    __shared__ u32x4 gs[128];
    u32x4 gv[5];
    if (wave < EARLY) {
#pragma unroll
        for (int j = 0; j < 5; ++j) gv[j] = __builtin_amdgcn_raw_buffer_load_b128(rg, c16 + j * 256, 0, 0);
#pragma unroll
        for (int j = 0; j < 5; ++j) asm volatile("" : "+v"(gv[j]));
    } else {
        if (wave < EARLY + 2) {
            const int t = tid - EARLY * 64;
            gs[t] = __builtin_amdgcn_raw_buffer_load_b128(rg, t * 16, 0, 0);
        }
        __syncthreads();
#pragma unroll
        for (int j = 0; j < 5; ++j) gv[j] = gs[j * 16 + (tid & 15)];
    }
    f32x2 gf[5][4];
#pragma unroll
    for (int j = 0; j < 5; ++j)
#pragma unroll
        for (int k = 0; k < 4; ++k) {
            gf[j][k] = f32x2{lo_f(gv[j][k]), hi_f(gv[j][k])};
            asm volatile("" : "+v"(gf[j][k]));
        }
    __builtin_amdgcn_sched_barrier(0);

    float ss0 = 0.f, ss1 = 0.f;
    f32x2 xf[5][4];
#pragma unroll
    for (int j = 0; j < 5; ++j) {
#pragma unroll
        for (int k = 0; k < 4; k += 2) {
            const unsigned ua = xv[j][k], ub = xv[j][k + 1];
            const bf16x2 a = __builtin_bit_cast(bf16x2, ua);
            const bf16x2 b = __builtin_bit_cast(bf16x2, ub);
            ss0 = __builtin_amdgcn_fdot2_f32_bf16(a, a, ss0, false);
            ss1 = __builtin_amdgcn_fdot2_f32_bf16(b, b, ss1, false);
        }
#pragma unroll
        for (int k = 0; k < 4; ++k) xf[j][k] = f32x2{lo_f(xv[j][k]), hi_f(xv[j][k])};
        if (j < 4) {
#pragma unroll
            for (int k = 0; k < 4; ++k) asm volatile("" : "+v"(xf[j][k]));
            asm volatile("" : "+v"(ss0), "+v"(ss1));
            __builtin_amdgcn_sched_barrier(0);
        }
    }
    const float ss = row16_allsum(ss0 + ss1);
    const float inv = __builtin_amdgcn_rsqf(ss * (1.0f / D) + 1e-6f);
    const f32x2 inv2 = {inv, inv};
#pragma unroll
    for (int j = 0; j < 5; ++j) {
        u32x4 o;
#pragma unroll
        for (int k = 0; k < 4; ++k) o[k] = pack_bf16((xf[j][k] * inv2) * gf[j][k]);
        __builtin_amdgcn_raw_buffer_store_b128(o, ro, voff + j * 256, 0, NT);
    }
    if (wave < EARLY) __builtin_amdgcn_s_barrier();
}

__global__ __launch_bounds__(256) void rmsnorm_generic(const unsigned short* __restrict__ x,
                                                       const unsigned short* __restrict__ g,
                                                       unsigned short* __restrict__ out, long long rows, long long d) {
    const long long row = (long long)blockIdx.x * 4 + (threadIdx.x >> 6);
    const int lane = threadIdx.x & 63;
    if (row >= rows) return;
    const unsigned short* xr = x + row * d;
    float ss = 0.f;
    for (long long c = lane; c < d; c += 64) {
        const float v = lo_f(xr[c]);
        ss = fmaf(v, v, ss);
    }
    for (int s = 32; s > 0; s >>= 1) ss += __shfl_xor(ss, s, 64);
    const float inv = __builtin_amdgcn_rsqf(ss / (float)d + 1e-6f);
    for (long long c = lane; c < d; c += 64) {
        const f32x2 v = {(lo_f(xr[c]) * inv) * lo_f(g[c]), 0.f};
        out[row * d + c] = (unsigned short)(pack_bf16(v) & 0xffffu);
    }
}

extern "C" void kernel_launch(void* const* d_in, const int* in_sizes, int n_in, void* d_out, int out_size,
                              void* d_ws, size_t ws_size, hipStream_t stream) {
    const long long n = in_sizes[0];
    const long long d = in_sizes[1];
    if (n <= 0 || d <= 0) return;
    const long long rows = n / d;
    if (d == D && n <= (1ll << 30)) {
        const unsigned groups = (unsigned)((rows + 3) / 4);
        const unsigned nblk = (groups + WPB - 1) / WPB;
        rmsnorm640<<<nblk, WPB * 64, 0, stream>>>(d_in[0], d_in[1], d_out, (unsigned)(n * 2), nblk * (4u * ROWB));
    } else {
        rmsnorm_generic<<<(unsigned)((rows + 3) / 4), 256, 0, stream>>>(
            (const unsigned short*)d_in[0], (const unsigned short*)d_in[1], (unsigned short*)d_out, rows, d);
    }
}
```
